```python
import torch

def _softmax(x, axis=-1):
    return torch.softmax(x, dim=axis)
def _gelu(x):
    return torch.nn.functional.gelu(x, approximate="tanh")
def _silu(x):
    return torch.nn.functional.silu(x)
def _sigmoid(x):
    return torch.sigmoid(x)
def _layernorm(x, axis=-1, eps=1e-5):
    m = x.mean(dim=axis, keepdim=True)
    v = x.var(dim=axis, keepdim=True, unbiased=False)
    return (x - m) / torch.sqrt(v + eps)
def _scatter_add(tgt, idx, src, axis=0):
    return tgt.clone().index_add_(axis, idx, src)
def _segment_sum(src, seg, n):
    out = torch.zeros((n,) + src.shape[1:], dtype=src.dtype)
    return out.index_add_(0, seg, src)
def _pad(x, pw):
    flat = tuple(v for pair in reversed(pw) for v in pair)
    return torch.nn.functional.pad(x, flat)

kernel_name = 'concat_heads__spectral_gfnet__ssm_conv_s4d__prompt'

def setup_inputs():

    _w = (lambda a: (lambda t: t.to(torch.bfloat16) if t.is_floating_point() else t)(torch.as_tensor(a)))
    def _A_setup():
        B, S, D = 1, 2048, 1024
        Df = D // 2 + 1
        return {
            "x": _w((lambda *s: torch.randn(*s, dtype=torch.bfloat16) * 0.05)(B, S, D)),
            "w_r": _w((lambda *s: torch.randn(*s, dtype=torch.bfloat16) * 0.8)(S, Df) * 0.02),
            "w_i": _w((lambda *s: torch.randn(*s, dtype=torch.bfloat16) * 0.8)(S, Df) * 0.02),
        }
    _A_in = _A_setup()

    def _B_setup():
        Bsz, L, D, N = 1, 2048, 1024, 128
        return {
            "u": _w((lambda *s: torch.randn(*s, dtype=torch.bfloat16) * 0.05)(Bsz, L, D)),
            "log_A_real": _w((lambda *s: torch.randn(*s, dtype=torch.bfloat16) * 6.4)(D, N) * 0.5),
            "A_imag": _w((lambda *s: torch.randn(*s, dtype=torch.bfloat16) * 0.05)(D, N) * 3.14),
            "C_real": _w((lambda *s: torch.randn(*s, dtype=torch.bfloat16) * 0.05)(D, N) / N ** 0.5),
            "C_imag": _w((lambda *s: torch.randn(*s, dtype=torch.bfloat16) * 0.05)(D, N) / N ** 0.5),
            "log_dt": _w((lambda *s: torch.randn(*s, dtype=torch.bfloat16) * 0.05)(D) * 0.5 - 4.0),
            "D_skip": _w((lambda *s: torch.randn(*s, dtype=torch.bfloat16) * 0.05)(D)),
        }
    _B_in = _B_setup()

    return {
        "A_x": _A_in["x"],
        "A_w_r": _A_in["w_r"],
        "A_w_i": _A_in["w_i"],
        "B_u": _B_in["u"],
        "B_log_A_real": _B_in["log_A_real"],
        "B_A_imag": _B_in["A_imag"],
        "B_C_real": _B_in["C_real"],
        "B_C_imag": _B_in["C_imag"],
        "B_log_dt": _B_in["log_dt"],
        "B_D_skip": _B_in["D_skip"],
    }

def reference(A_x, A_w_r, A_w_i, B_u, B_log_A_real, B_A_imag, B_C_real, B_C_imag, B_log_dt, B_D_skip):
    def _A_ref(x, w_r, w_i):
        B, S, D = x.shape
        X = (lambda x, axes=(-2,-1): torch.fft.rfft2(x.float(), dim=axes))(x, axes=(-2, -1))
        W = w_r + 1j * w_i
        X = X * W
        y = (lambda x, s, axes=(-2,-1): torch.fft.irfft2(x, s=s, dim=axes).to(torch.bfloat16))(X, (S, D), axes=(-2, -1))
        return y

    def _B_ref(u, log_A_real, A_imag, C_real, C_imag, log_dt, D_skip):
        L = u.shape[1]
        dt = torch.exp(log_dt)
        A = -torch.exp(log_A_real) + 1j * A_imag
        C = (C_real + 1j * C_imag) * dt[:, None]
        pos = (lambda x, dt: x.to(dt))(torch.arange(L), log_dt.dtype)
        P = torch.exp((A * dt[:, None])[:, :, None] * pos[None, None, :])
        K = 2.0 * torch.einsum("dn,dnl->dl", C, P).real
        L2 = 2 * L
        u_t = (lambda x, *d: x.permute(*d))(u, 0, 2, 1)
        Kf = (lambda x, *a, **k: torch.fft.rfft(x.float(), *a, **k))(K, L2)
        Uf = (lambda x, *a, **k: torch.fft.rfft(x.float(), *a, **k))(u_t, L2)
        y = (lambda x, *a, **k: torch.fft.irfft(x, *a, **k).to(torch.bfloat16))(Uf * Kf[None, :, :], L2)[:, :, :L]
        y = (lambda x, *d: x.permute(*d))(y, 0, 2, 1)
        return y + u * D_skip[None, None, :]

    a = _A_ref(A_x, A_w_r, A_w_i)
    b = _B_ref(B_u, B_log_A_real, B_A_imag, B_C_real, B_C_imag, B_log_dt, B_D_skip)
    return torch.concatenate([a, b], axis=-1)
```

```cpp
#include <hip/hip_runtime.h>
#include <hip/hip_bf16.h>
#include <stdint.h>

typedef _Float16 h8 __attribute__((ext_vector_type(8)));
typedef _Float16 h2 __attribute__((ext_vector_type(2)));
typedef float f4 __attribute__((ext_vector_type(4)));
typedef __bf16 bf2 __attribute__((ext_vector_type(2)));
typedef unsigned int u32;
typedef u32 u32x2 __attribute__((ext_vector_type(2)));
typedef u32 u32x4 __attribute__((ext_vector_type(4)));

#define DEV __device__ __forceinline__
#if defined(__HIP_DEVICE_COMPILE__)
#define NOPK __attribute__((target("no-packed-fp32-ops")))
#else
#define NOPK
#endif

struct cf { float x, y; };
DEV cf operator+(cf a, cf b) { return cf{a.x + b.x, a.y + b.y}; }
DEV cf operator-(cf a, cf b) { return cf{a.x - b.x, a.y - b.y}; }
DEV cf cmul(cf a, cf b) { return cf{a.x * b.x - a.y * b.y, a.x * b.y + a.y * b.x}; }
DEV cf cmulc(cf a, cf b) { return cf{a.x * b.x + a.y * b.y, a.y * b.x - a.x * b.y}; }
DEV cf cscale(cf a, float s) { return cf{a.x * s, a.y * s}; }
DEV cf csel(bool z, cf a, cf b) { float x = z ? a.x : b.x, y = z ? a.y : b.y; return cf{x, y}; }

DEV float bf_lo(u32 w) { return __builtin_bit_cast(float, w << 16); }
DEV float bf_hi(u32 w) { return __builtin_bit_cast(float, w & 0xffff0000u); }
DEV u32 pack_bf(float a, float b) { bf2 v = {(__bf16)a, (__bf16)b}; return __builtin_bit_cast(u32, v); }
DEV float bf_round(float a) { return bf_lo(pack_bf(a, 0.f) & 0xffffu); }
DEV u32 pack_h(float a, float b) { h2 v = {(_Float16)a, (_Float16)b}; return __builtin_bit_cast(u32, v); }
DEV float pow2i(int e) { return __builtin_bit_cast(float, (u32)(e + 127) << 23); }
DEV int fexp(float m) { return (int)((__builtin_bit_cast(u32, m) >> 23) & 0xff) - 127; }
DEV int clampi(int v, int lo, int hi) { return v < lo ? lo : (v > hi ? hi : v); }
DEV void wave_sync() {
  __builtin_amdgcn_fence(__ATOMIC_ACQ_REL, "wavefront");
  __builtin_amdgcn_wave_barrier();
}
template <int CTRL> DEV int dppi(int v) { return __builtin_amdgcn_mov_dpp(v, CTRL, 0xf, 0xf, true); }
DEV int wave_maxi(int v) {
  v = max(v, dppi<0x128>(v)); v = max(v, dppi<0x124>(v)); v = max(v, dppi<0x122>(v)); v = max(v, dppi<0x121>(v));
  return max(max(__builtin_amdgcn_readlane(v, 0), __builtin_amdgcn_readlane(v, 16)),
             max(__builtin_amdgcn_readlane(v, 32), __builtin_amdgcn_readlane(v, 48)));
}
DEV float wave_max(float v) { return __builtin_bit_cast(float, wave_maxi(__builtin_bit_cast(int, v))); }
template <int CTRL> DEV float dppf(float v) { return __builtin_bit_cast(float, dppi<CTRL>(__builtin_bit_cast(int, v))); }
DEV float rdl(float v, int l) { return __builtin_bit_cast(float, __builtin_amdgcn_readlane(__builtin_bit_cast(int, v), l)); }
DEV float wave_sum(float v) {
  v += dppf<0x128>(v); v += dppf<0x124>(v); v += dppf<0x122>(v); v += dppf<0x121>(v);
  return (rdl(v, 0) + rdl(v, 16)) + (rdl(v, 32) + rdl(v, 48));
}
DEV u32 dpp_xor1(u32 v) { return (u32)__builtin_amdgcn_mov_dpp((int)v, 0xB1, 0xf, 0xf, true); }

typedef __bf16 b8 __attribute__((ext_vector_type(8)));
DEV int pi4(int i) { return (i >> 2) + 4 * (i & 3); }
#define HCOS __builtin_amdgcn_cosf
#define HSIN __builtin_amdgcn_sinf
DEV h8 genF16(int lane, int ph, int v) {
  const int n = lane & 15, q = lane >> 4;
  const float st = (float)(4 * n + v) * (1.f / 64.f);
  u32x4 o;
#pragma unroll
  for (int ap = 0; ap < 4; ++ap) {
    const float rev = __builtin_amdgcn_fractf((float)(4 * q + ap) * st) + 0.25f * (float)ph;
    o[ap] = pack_h(HCOS(rev), HSIN(rev));
  }
  return __builtin_bit_cast(h8, o);
}
DEV h8 genF16B(int lane, int ph, int hl) {
  const int n = lane & 15, q = lane >> 4;
  u32x4 o;
#pragma unroll
  for (int ap = 0; ap < 4; ++ap) {
    const float rev = (float)(((4 * q + ap) * n) & 15) * (1.f / 16.f) + 0.25f * (float)ph;
    const float c = HCOS(rev), sn = HSIN(rev);
    const u32 hi = pack_bf(c, sn);
    const u32 lo = pack_bf(c - bf_lo(hi), sn - bf_hi(hi));
    o[ap] = hl ? lo : hi;
  }
  return __builtin_bit_cast(h8, o);
}
DEV h8 genF32(int lane, int k, int v) {
  const int kb = 16 * (k >> 2) + pi4(lane & 15), r0 = 16 * (k & 1) + (lane >> 4);
  u32x4 o;
#pragma unroll
  for (int rg = 0; rg < 4; ++rg) {
    const float rev = (float)((kb * (4 * (r0 + 4 * rg) + v) + 32 * ((k >> 1) & 1)) & 127) * (1.f / 128.f);
    o[rg] = pack_h(HCOS(rev), HSIN(rev));
  }
  return __builtin_bit_cast(h8, o);
}
DEV f4 genTW(int j0, int j1, float sc) {
  const float a = (float)(j0 & 2047) * (1.f / 2048.f), b = (float)(j1 & 2047) * (1.f / 2048.f);
  return f4{HCOS(a) * sc, -HSIN(a) * sc, HCOS(b) * sc, -HSIN(b) * sc};
}
DEV f4 mfma_h(h8 a, h8 b, f4 c) { return __builtin_amdgcn_mfma_f32_16x16x32_f16(a, b, c, 0, 0, 0); }
DEV f4 mfma_b(h8 a, h8 b, f4 c) {
  return __builtin_amdgcn_mfma_f32_16x16x32_bf16(__builtin_bit_cast(b8, a), __builtin_bit_cast(b8, b), c, 0, 0, 0);
}
DEV h8 twpack(f4 dr, f4 di, f4 t01, f4 t23) {
  u32x4 o;
  o.x = pack_h(dr[0] * t01.x - di[0] * t01.y, dr[0] * t01.y + di[0] * t01.x);
  o.y = pack_h(dr[1] * t01.z - di[1] * t01.w, dr[1] * t01.w + di[1] * t01.z);
  o.z = pack_h(dr[2] * t23.x - di[2] * t23.y, dr[2] * t23.y + di[2] * t23.x);
  o.w = pack_h(dr[3] * t23.z - di[3] * t23.w, dr[3] * t23.w + di[3] * t23.z);
  return __builtin_bit_cast(h8, o);
}
#define Z4 f4{0.f, 0.f, 0.f, 0.f}
#define ST_AUX 16
typedef __amdgpu_buffer_rsrc_t rsrc_t;
DEV rsrc_t mkrsrc(void* p) { return __builtin_amdgcn_make_buffer_rsrc(p, 0, 0x7fffffff, 0x00020000); }
#define PRIO_A 2
#define PRIO_B0 3
#define PRIO_B1 0
#define ST_AUX_B 16
#define ST_AUX_OUTB 16
#define ST_AUX_OUTA 0
template <int AUX = ST_AUX> DEV void st128(rsrc_t r, u32 dw_off, u32x4 v) { __builtin_amdgcn_raw_buffer_store_b128(v, r, dw_off * 4u, 0, AUX); }
template <int AUX = ST_AUX> DEV void st64(rsrc_t r, u32 dw_off, u32x2 v) { __builtin_amdgcn_raw_buffer_store_b64(v, r, dw_off * 4u, 0, AUX); }

DEV f4 fsel(bool c, f4 a, f4 b) { return f4{c ? a.x : b.x, c ? a.y : b.y, c ? a.z : b.z, c ? a.w : b.w}; }
DEV u32x2 lds_rd64(const char* p) {
  return *(const volatile __attribute__((address_space(3))) u32x2*)(p);
}
DEV u32 lds_rd16(const unsigned short* p) {
  return *(const volatile __attribute__((address_space(3))) unsigned short*)(p);
}
DEV h8 ldfrag2(const char* p0, const char* p1) {
  const u32x2 a = lds_rd64(p0), b = lds_rd64(p1);
  return __builtin_bit_cast(h8, u32x4{a.x, a.y, b.x, b.y});
}
static constexpr size_t WS_V1 = 0;
static constexpr size_t WS_V2 = WS_V1 + (4u << 20);
static constexpr size_t WS_UT = WS_V2 + (4u << 20);
static constexpr size_t WS_YT = WS_UT + (4u << 20);
static constexpr size_t WS_KG = WS_YT + (4u << 20);
static constexpr size_t WS_S1 = WS_KG + (4u << 20);
static constexpr size_t WS_S2 = WS_S1 + 4096;
static constexpr size_t WS_KI = WS_S2 + 4096;

static constexpr int AC_RP = 80;
static constexpr int AC_CP = 32 * AC_RP + 32;
static constexpr int AC_IN = 8 * AC_CP;
static constexpr int AC_WP = 2048 + 32;
static constexpr int AC_CT = AC_IN + 8 * AC_WP + 64;
static constexpr int AC_LDS = AC_CT + 16 * 1024;

template <bool FWD>
DEV void roleAC(int bid, const u32* __restrict__ src, const int* __restrict__ ein, u32* __restrict__ dst, int* __restrict__ eout,
                char* smem, const u32* __restrict__ wr32 = nullptr, const u32* __restrict__ wi32 = nullptr) {
  const int tid = threadIdx.x, lane = tid & 63, wv = tid >> 6;
  const int xcd = bid & 7, idx = bid >> 3;
  const int cg = xcd * 8 + (idx & 7), s_lo = idx >> 3;
  const int hh = tid & 1;
  const int blk = s_lo * 64 + cg;
  u32x4 ld[2];
  int ex[2], nn[2];
  int emax = 0;
  if (!FWD) {
    const u32x4 e0 = *(const u32x4*)(ein + 4 * lane), e1 = *(const u32x4*)(ein + 256 + 4 * lane);
    emax = max(max(max((int)e0.x, (int)e0.y), max((int)e0.z, (int)e0.w)), max(max((int)e1.x, (int)e1.y), max((int)e1.z, (int)e1.w)));
  }
#pragma unroll
  for (int it = 0; it < 2; ++it) {
    const int r = (tid >> 5) + 16 * it;
    if (FWD) {
      nn[it] = 32 * ((tid >> 1) & 15) + r;
      ld[it] = *(const u32x4*)(src + (size_t)(s_lo + 4 * nn[it]) * 512 + 8 * cg + 4 * hh);
    } else {
      const int m = (tid >> 1) & 1, pp = 32 * ((tid >> 2) & 7) + r;
      nn[it] = m ? (pp ? 512 - pp : 256) : pp;
      ex[it] = ein[nn[it]];
      ld[it] = *(const u32x4*)(src + (size_t)blk * 4096 + pp * 16 + 8 * m + 4 * hh);
    }
  }
  {
    char* ct = smem + AC_CT + lane * 16;
    *(h8*)(ct + wv * 1024) = genF32(lane, wv, FWD ? s_lo : 0);
    if (wv < 4) {
      const int n = lane & 15, rr = 16 * (wv >> 1) + (lane >> 4) + 8 * (wv & 1);
      const int j0 = FWD ? n * (4 * rr + s_lo) : rr * (4 * n + s_lo);
      const int j1 = FWD ? n * (4 * (rr + 4) + s_lo) : (rr + 4) * (4 * n + s_lo);
      *(f4*)(ct + (12 + wv) * 1024) = genTW(j0, j1, 1.f / 512.f);
    } else if (FWD) {
      *(h8*)(ct + (4 + wv) * 1024) = genF16B(lane, wv & 1, (wv >> 1) & 1);
    } else if (wv < 6) {
      *(h8*)(ct + (4 + wv) * 1024) = genF16(lane, wv & 1, s_lo);
    }
  }
  float* red = (float*)(smem + AC_IN + 8 * AC_WP);
  if (FWD) {
    float m = 0.f;
#pragma unroll
    for (int it = 0; it < 2; ++it)
#pragma unroll
      for (int cc = 0; cc < 4; ++cc) m = fmaxf(m, fmaxf(fabsf(bf_lo(ld[it][cc])), fabsf(bf_hi(ld[it][cc]))));
    m = wave_max(m);
    if (lane == 0) red[wv] = m;
  } else {
    emax = wave_maxi(emax);
#pragma unroll
    for (int it = 0; it < 2; ++it) {
      const float rt = pow2i(max(ex[it] - emax, -40));
      const h2 rr = __builtin_bit_cast(h2, pack_h(rt, -rt));
#pragma unroll
      for (int cc = 0; cc < 4; ++cc) { const u32 t = ld[it][cc]; ld[it][cc] = __builtin_bit_cast(u32, __builtin_bit_cast(h2, t) * rr); }
    }
  }
#pragma unroll
  for (int it = 0; it < 2; ++it) {
    char* dp = smem + (4 * hh) * AC_CP + ((nn[it] & 16) + pi4(nn[it] & 15)) * AC_RP + 4 * (nn[it] >> 5);
#pragma unroll
    for (int cc = 0; cc < 4; ++cc) *(u32*)(dp + cc * AC_CP) = ld[it][cc];
  }
  u32x4 pf0, pf1;
  if (FWD) {
    const int p = (bid & 7) * 32 + (bid >> 3);
    const int kh = (wv & 4) ? (p ? 512 - p : 256) : p;
    const int o = ((kh + 512 * (wv & 3)) * 513) >> 1;
    pf0 = *(const u32x4*)(wr32 + o + 4 * lane); pf1 = *(const u32x4*)(wi32 + o + 4 * lane);
  }
  __syncthreads();
  h8 F1[4], F2[8];
  f4 tw[4];
  {
    const char* ct = smem + AC_CT + lane * 16;
#pragma unroll
    for (int k = 0; k < (FWD ? 4 : 2); ++k) F1[k] = *(const h8*)(ct + (8 + k) * 1024);
#pragma unroll
    for (int k = 0; k < 4; ++k) tw[k] = *(const f4*)(ct + (12 + k) * 1024);
#pragma unroll
    for (int k = 0; k < 8; ++k) F2[k] = *(const h8*)(ct + k * 1024);
  }
  float osc = 1.f;
  if (FWD) {
    const f4 r0 = *(const f4*)red, r1 = *(const f4*)(red + 4);
    const float m = fmaxf(fmaxf(fmaxf(r0.x, r0.y), fmaxf(r0.z, r0.w)), fmaxf(fmaxf(r1.x, r1.y), fmaxf(r1.z, r1.w)));
    const int se = clampi(13 - fexp(m), -100, 100);
    const float sc = pow2i(se);
#pragma unroll
    for (int k = 0; k < 4; ++k) tw[k] *= sc;
    if (tid == 0) eout[s_lo * 64 + cg] = 9 - se;
  } else {
    osc = pow2i(clampi(emax + 9, -126, 127));
  }
  char* ob = smem + AC_IN;
  {
    h8 B[2];
#pragma unroll
    for (int mt = 0; mt < 2; ++mt) {
      const h8 A = *(const h8*)(smem + wv * AC_CP + (16 * mt + (lane & 15)) * AC_RP + 16 * (lane >> 4));
      f4 dr, di;
      if (FWD) {
        dr = mfma_b(A, F1[2], Z4); di = mfma_b(A, F1[3], Z4);
        dr = mfma_b(A, F1[0], dr); di = mfma_b(A, F1[1], di);
      } else {
        dr = mfma_h(A, F1[0], Z4); di = mfma_h(A, F1[1], Z4);
      }
      B[mt] = twpack(dr, di, tw[2 * mt], tw[2 * mt + 1]);
    }
#pragma unroll
    for (int ot = 0; ot < 2; ++ot) {
      f4 yr = mfma_h(F2[4 * ot + 0], B[0], Z4), yi = mfma_h(F2[4 * ot + 2], B[0], Z4);
      yr = mfma_h(F2[4 * ot + 1], B[1], yr); yi = mfma_h(F2[4 * ot + 3], B[1], yi);
#pragma unroll
      for (int rg = 0; rg < 4; ++rg)
        *(u32*)(ob + wv * AC_WP + (lane + 64 * (4 * ot + rg)) * 4) = FWD ? pack_h(yr[rg], yi[rg]) : pack_bf(yr[rg] * osc, -yi[rg] * osc);
    }
  }
  __syncthreads();
#pragma unroll
  for (int it = 0; it < 2; ++it) {
    const int t2 = (tid >> 1) + 256 * it;
    int k = t2;
    if (FWD) { const int pp = t2 >> 1; k = (t2 & 1) ? (pp ? 512 - pp : 256) : pp; }
    const char* sp = ob + (4 * hh) * AC_WP + k * 4;
    const u32x4 o = u32x4{*(const u32*)sp, *(const u32*)(sp + AC_WP), *(const u32*)(sp + 2 * AC_WP), *(const u32*)(sp + 3 * AC_WP)};
    if (FWD) st128(mkrsrc(dst), blk * 4096 + t2 * 8 + 4 * hh, o);
    else st128<ST_AUX_OUTA>(mkrsrc(dst), (4 * k + s_lo) * 1024 + 8 * cg + 4 * hh, o);
  }
  if (FWD) asm volatile("" :: "v"(pf0), "v"(pf1));
}

static constexpr int A2_RB = 2576;
static constexpr int A2_U = 8 * A2_RB;
static constexpr int A2_WP = 1056;
static constexpr int A2_W = 16 * A2_WP;
static constexpr int A2_CT = 22 * 1024;
static constexpr int A2_X = 8 * 2048;
static constexpr int A2_LDS = A2_U + A2_W + A2_CT + A2_X + 128;
DEV h2 h2_mni(h2 a) { return h2{a.y, -a.x}; }
DEV h2 h2_pi(h2 a) { return h2{-a.y, a.x}; }
DEV h2 as_h2(u32 v) { return __builtin_bit_cast(h2, v); }
DEV u32 as_u(h2 v) { return __builtin_bit_cast(u32, v); }
DEV h8 h8_scale(h8 v, float f) { const _Float16 g = (_Float16)f; return v * h8{g, g, g, g, g, g, g, g}; }

struct A2C { h8 F1[2], F2[8]; f4 tw[4]; };
DEV void a2_fwd(const char* row, u32 cm, int n16, int q, const A2C& C, f4 (&yr)[2], f4 (&yi)[2]) {
  h8 T[2];
#pragma unroll
  for (int mt = 0; mt < 2; ++mt) {
    const h8 A = __builtin_bit_cast(h8, *(const u32x4*)(row + (16 * mt + n16) * 80 + 16 * q) ^ cm);
    T[mt] = twpack(mfma_h(A, C.F1[0], Z4), mfma_h(A, C.F1[1], Z4), C.tw[2 * mt], C.tw[2 * mt + 1]);
  }
#pragma unroll
  for (int ot = 0; ot < 2; ++ot) {
    yr[ot] = mfma_h(C.F2[4 * ot + 1], T[1], mfma_h(C.F2[4 * ot + 0], T[0], Z4));
    yi[ot] = mfma_h(C.F2[4 * ot + 3], T[1], mfma_h(C.F2[4 * ot + 2], T[0], Z4));
  }
}
struct A2K { cf al[4], de[4], be[4]; };
DEV void a2_coef(int ot, int lane, const unsigned short* wa, const unsigned short* wb, f4 tp0, f4 tp1, A2K& K) {
#pragma unroll
  for (int rg = 0; rg < 4; ++rg) {
    cf W1 = cf{bf_lo(wa[64 * rg]), bf_lo(wa[64 * rg + A2_WP / 2])};
    cf W2 = cf{bf_lo(wb[-64 * rg]), bf_lo(wb[-64 * rg + A2_WP / 2])};
    if (rg == 0) {
      const bool z = (lane == 0) && (ot == 0);
      const cf Wx0 = cf{bf_lo(wb[z ? -512 : 0]), bf_lo(wb[(z ? -512 : 0) + A2_WP / 2])};
      const cf Wx1 = cf{bf_lo(wa[z ? 512 : 0]), bf_lo(wa[(z ? 512 : 0) + A2_WP / 2])};
      W1 = csel(z, cf{0.5f * (W1.x + Wx0.x), 0.5f * (W1.y - Wx0.y)}, W1);
      W2 = csel(z, cf{0.5f * (Wx1.x + W2.x), 0.5f * (W2.y - Wx1.y)}, W2);
    }
    const f4 tp = rg < 2 ? tp0 : tp1;
    const float c = (rg & 1) ? tp.z : tp.x, sn = (rg & 1) ? tp.w : tp.y;
    const cf Sg = cf{W1.x + W2.x, W1.y - W2.y}, Dl = cf{W1.x - W2.x, W1.y + W2.y};
    K.al[rg] = cf{Sg.x + c * Dl.x, Sg.y + c * Dl.y};
    K.de[rg] = cf{Sg.x - c * Dl.x, Sg.y - c * Dl.y};
    K.be[rg] = cf{sn * Dl.y, -sn * Dl.x};
  }
}
DEV float a2_point(f4 pr, f4 pi, f4 qr, f4 qi, const A2K& K, cf (&o1)[4], cf (&o2)[4]) {
  float m = 0.f;
#pragma unroll
  for (int rg = 0; rg < 4; ++rg) {
    const cf P = cf{pr[rg], pi[rg]}, Qc = cf{qr[rg], qi[rg]};
    o1[rg] = cmul(K.al[rg], P) + cmul(K.be[rg], Qc);
    o2[rg] = cmul(K.de[rg], Qc) - cmul(K.be[rg], P);
    m = fmaxf(m, fmaxf(fmaxf(fabsf(o1[rg].x), fabsf(o1[rg].y)), fmaxf(fabsf(o2[rg].x), fabsf(o2[rg].y))));
  }
  return m;
}
DEV h8 a2_packc(const cf (&o)[4], float sc) {
  u32x4 a;
#pragma unroll
  for (int rg = 0; rg < 4; ++rg) a[rg] = pack_h(o[rg].x * sc, o[rg].y * -sc);
  return __builtin_bit_cast(h8, a);
}

DEV void roleA2(int bid, const u32* V1, const int* __restrict__ e1, const unsigned short* __restrict__ wr,
                const unsigned short* __restrict__ wi, u32* V2, int* __restrict__ e2, char* smem) {
  const int p = (bid & 7) * 32 + (bid >> 3);
  const int tid = threadIdx.x, lane = tid & 63;
  const int wv = __builtin_amdgcn_readfirstlane(tid >> 6);
  char* UU = smem;
  char* WS = smem + A2_U;
  char* CT = smem + A2_U + A2_W;
  char* XA = CT + A2_CT;
  char* XB = CT + 12 * 1024;
  int* ew = (int*)(XA + A2_X);
  const int kh0 = p, kh1 = p == 0 ? 256 : 512 - p;
  const int cgr = ((tid >> 3) & 7) * 4 + ((tid >> 6) & 3) + 32 * (tid >> 8);
  const int g = (tid >> 2) & 1, c2 = 4 * cgr + (tid & 3);
  const int kh = g ? kh1 : kh0;
  u32x2 raw[4];
  int ex[4];
#pragma unroll
  for (int s = 0; s < 4; ++s) {
    raw[s] = *(const u32x2*)(V1 + (size_t)(s * 64 + cgr) * 4096 + p * 16 + 2 * (tid & 7));
    ex[s] = e1[s * 64 + cgr];
  }
  const u32x4 eall = *(const u32x4*)(e1 + 4 * lane);
  const int un = wv >> 1, side = wv & 1;
  int slot;
  bool self = false;
  if (p != 0) slot = side ? 7 - un : un;
  else if (un == 0) slot = side ? 3 : 1;
  else if (un == 1) slot = side ? 7 : 4;
  else if (un == 2) slot = side ? 6 : 5;
  else { slot = side ? 2 : 0; self = true; }
  const int bmask = p != 0 ? 0xF0 : 0xC8;
  const int k1 = ((slot & 4) ? kh1 : kh0) + 512 * (slot & 3);
  const int wo = (k1 * 513) >> 1;
  const u32x4 wl0 = *(const u32x4*)((const u32*)wr + wo + 4 * lane), wl1 = *(const u32x4*)((const u32*)wi + wo + 4 * lane);
  const int tl = lane < 6 ? lane : 0;
  const unsigned short wt = (tl >= 3 ? wi : wr)[k1 * 513 + 510 + (tl >= 3 ? tl - 3 : tl)];
  const int n16 = lane & 15, q = lane >> 4;
  A2C C;
  {
    char* ct = CT + lane * 16;
    *(h8*)(ct + wv * 1024) = genF32(lane, wv, 0);
    const int w4 = wv & 3;
    if (wv < 4) {
      const int r2 = 16 * (w4 >> 1) + pi4(n16), ka = 4 * q + 2 * (w4 & 1);
      *(f4*)(ct + (8 + w4) * 1024) = genTW(4 * r2 * ka, 4 * r2 * (ka + 1), 1.f / 512.f);
      const int kb0 = 2 * (lane + 128 * w4) + 512;
      *(f4*)(ct + (18 + w4) * 1024) = genTW(kb0, kb0 + 128, 1.f);
    } else {
      const int rr = 16 * (w4 >> 1) + q + 8 * (w4 & 1);
      *(f4*)(ct + (14 + w4) * 1024) = genTW(4 * n16 * rr, 4 * n16 * (rr + 4), 1.f / 512.f);
      if (w4 < 2) *(h8*)(ct + (12 + w4) * 1024) = genF16(lane, w4, 0);
    }
  }
  const int emax = wave_maxi(max(max((int)eall.x, (int)eall.y), max((int)eall.z, (int)eall.w)));
  __builtin_amdgcn_s_setprio(PRIO_A);
  {
    h2 rt[4];
#pragma unroll
    for (int s = 0; s < 4; ++s) { const float f = pow2i(max(ex[s] - emax - 2, -40)); rt[s] = as_h2(pack_h(f, f)); }
    const int a = c2 >> 4, r0 = 2 * (c2 & 15);
    char* dst = UU + g * 4 * A2_RB + ((r0 & 16) + pi4(r0 & 15)) * 80 + 4 * a;
#pragma unroll
    for (int j = 0; j < 2; ++j) {
      const u32 w0 = raw[0][j], w1 = raw[1][j], w2 = raw[2][j], w3 = raw[3][j];
      const h2 v0 = as_h2(w0) * rt[0], v1 = as_h2(w1) * rt[1], v2 = as_h2(w2) * rt[2], v3 = as_h2(w3) * rt[3];
      const h2 t0 = v0 + v2, t1 = v0 - v2, t2 = v1 + v3, t3 = h2_mni(v1 - v3);
      *(u32*)(dst + j * 320) = as_u(t0 + t2);
      *(u32*)(dst + j * 320 + A2_RB) = as_u(t1 + t3);
      *(u32*)(dst + j * 320 + 2 * A2_RB) = as_u(t0 - t2);
      *(u32*)(dst + j * 320 + 3 * A2_RB) = as_u(t1 - t3);
    }
  }
  {
    char* wsp = WS + wv * 2 * A2_WP;
    *(u32x4*)(wsp + 16 * lane) = wl0;
    *(u32x4*)(wsp + A2_WP + 16 * lane) = wl1;
    wave_sync();
    if (lane < 6) *(unsigned short*)(wsp + (lane >= 3 ? A2_WP : 0) + 2 * (510 + (lane >= 3 ? lane - 3 : lane) + (k1 & 1))) = wt;
  }
  __syncthreads();
#pragma unroll
  for (int k = 0; k < 2; ++k) C.F1[k] = *(const h8*)(CT + (12 + k) * 1024 + lane * 16);
#pragma unroll
  for (int k = 0; k < 4; ++k) C.tw[k] = *(const f4*)(CT + (14 + k) * 1024 + lane * 16);
#pragma unroll
  for (int k = 0; k < 8; ++k) C.F2[k] = *(const h8*)(CT + k * 1024 + lane * 16);
  char* row = UU + slot * A2_RB;
  h8 PI[2];
  int eW;
  if (!self) {
    f4 yr[2], yi[2];
    a2_fwd(row, side ? 0x80000000u : 0u, n16, q, C, yr, yi);
    *(f4*)(XA + wv * 2048 + lane * 16) = fsel(side, yr[0], yr[1]);
    *(f4*)(XA + wv * 2048 + 1024 + lane * 16) = fsel(side, yi[0], yi[1]);
    const f4 mr = fsel(side, yr[1], yr[0]), mi = fsel(side, yi[1], yi[0]);
    const unsigned short* wa = (const unsigned short*)(WS + (wv & ~1) * 2 * A2_WP) + (k1 & 1) + lane + 256 * side;
    const unsigned short* wb = (const unsigned short*)(WS + (wv | 1) * 2 * A2_WP) + (k1 & 1) + 512 - lane - 256 * side;
    A2K K;
    a2_coef(side, lane, wa, wb, *(const f4*)(CT + (18 + 2 * side) * 1024 + lane * 16), *(const f4*)(CT + (19 + 2 * side) * 1024 + lane * 16), K);
    if (side) {
#pragma unroll
      for (int rg = 0; rg < 4; ++rg) { const cf t = K.al[rg]; K.al[rg] = K.de[rg]; K.de[rg] = t; K.be[rg] = cf{-K.be[rg].x, -K.be[rg].y}; }
    }
    __syncthreads();
    const f4 orr = *(const f4*)(XA + (wv ^ 1) * 2048 + lane * 16), oi = *(const f4*)(XA + (wv ^ 1) * 2048 + 1024 + lane * 16);
    cf o1[4], o2[4];
    float m = a2_point(mr, mi, orr, oi, K, o1, o2);
    m = wave_max(m);
    const int em = clampi(13 - fexp(m), -100, 100);
    const float sW = pow2i(em);
    *(h8*)(XB + wv * 1024 + lane * 16) = a2_packc(o2, sW);
    if (lane == 0) ew[8 + wv] = em;
    h8 own = a2_packc(o1, sW);
    __syncthreads();
    h8 oth = *(const h8*)(XB + (wv ^ 1) * 1024 + lane * 16);
    const int eo = ew[8 + (wv ^ 1)];
    eW = min(em, eo);
    own = h8_scale(own, pow2i(max(eW - em, -40)));
    oth = h8_scale(oth, pow2i(max(eW - eo, -40)));
    PI[0] = side ? oth : own; PI[1] = side ? own : oth;
  } else {
    f4 pr[2], pi[2], qr[2], qi[2], tp[4];
#pragma unroll
    for (int k = 0; k < 4; ++k) tp[k] = *(const f4*)(CT + (18 + k) * 1024 + lane * 16);
    a2_fwd(row, 0u, n16, q, C, pr, pi);
    __syncthreads();
    a2_fwd(row, 0x80000000u, n16, q, C, qr, qi);
    const unsigned short* wa = (const unsigned short*)(WS + wv * 2 * A2_WP) + lane;
    const unsigned short* wb = (const unsigned short*)(WS + wv * 2 * A2_WP) + 512 - lane;
    cf o1[2][4], o2[4];
    A2K K;
    a2_coef(0, lane, wa, wb, tp[0], tp[1], K);
    float m = a2_point(pr[0], pi[0], qr[0], qi[0], K, o1[0], o2);
    __syncthreads();
    a2_coef(1, lane, wa + 256, wb - 256, tp[2], tp[3], K);
    m = fmaxf(m, a2_point(pr[1], pi[1], qr[1], qi[1], K, o1[1], o2));
    m = wave_max(m);
    eW = clampi(13 - fexp(m), -100, 100);
    PI[0] = a2_packc(o1[0], pow2i(eW)); PI[1] = a2_packc(o1[1], pow2i(eW));
  }
  if (lane == 0) ew[slot] = eW;
#pragma unroll
  for (int nt = 0; nt < 2; ++nt) {
    f4 ar = mfma_h(PI[1], C.F2[4 * nt + 1], mfma_h(PI[0], C.F2[4 * nt + 0], Z4));
    f4 ai = mfma_h(PI[1], C.F2[4 * nt + 3], mfma_h(PI[0], C.F2[4 * nt + 2], Z4));
    const f4 t0 = *(const f4*)(CT + (8 + 2 * nt) * 1024 + lane * 16), t1 = *(const f4*)(CT + (9 + 2 * nt) * 1024 + lane * 16);
    const h8 X = twpack(ar, ai, t0, t1);
    ar = mfma_h(C.F1[0], X, Z4); ai = mfma_h(C.F1[1], X, Z4);
    const int oo = 4 * (128 * q + 16 * nt + pi4(n16));
#pragma unroll
    for (int rg = 0; rg < 4; ++rg) *(u32*)(row + oo + 128 * rg) = pack_h(ar[rg], ai[rg]);
  }
  __syncthreads();
  {
    const u32x4 ewg = *(const u32x4*)(ew + 4 * g);
    const int emin = min(min((int)ewg.x, (int)ewg.y), min((int)ewg.z, (int)ewg.w));
    if (c2 == 0) e2[kh] = emax - emin + 1;
    u32x2 uu[4];
    h2 rt[4];
#pragma unroll
    for (int kl = 0; kl < 4; ++kl) {
      uu[kl] = *(const u32x2*)(UU + (g * 4 + kl) * A2_RB + 8 * c2);
      const float f = pow2i(max(emin - (int)ewg[kl] - 2, -40));
      rt[kl] = as_h2(pack_h(f, ((bmask >> (4 * g + kl)) & 1) ? f : -f));
    }
    u32x2 o[4];
#pragma unroll
    for (int j = 0; j < 2; ++j) {
      const u32 w0 = uu[0][j], w1 = uu[1][j], w2 = uu[2][j], w3 = uu[3][j];
      const h2 v0 = as_h2(w0) * rt[0], v1 = as_h2(w1) * rt[1], v2 = as_h2(w2) * rt[2], v3 = as_h2(w3) * rt[3];
      const h2 t0 = v0 + v2, t1 = v0 - v2, t2 = v1 + v3, t3 = h2_pi(v1 - v3);
      o[0][j] = as_u(t0 + t2); o[1][j] = as_u(t1 + t3); o[2][j] = as_u(t0 - t2); o[3][j] = as_u(t1 - t3);
    }
#pragma unroll
    for (int s = 0; s < 4; ++s) st64(mkrsrc(V2), (s * 64 + cgr) * 4096 + p * 16 + 2 * (tid & 7), o[s]);
  }
}

static constexpr int KG_PITCH = 272;
static constexpr int KG_BUF = 16 * KG_PITCH;
static constexpr int KG_WAVE = KG_BUF + 2608;
static constexpr int KC_DW = 336;
static constexpr int B1_LDS = 8 * KG_WAVE + 64 + 16384;

DEV cf cexp_p(float al, float th, float pp) {
  float mag = __builtin_amdgcn_exp2f(al * pp);
  float rev = __builtin_amdgcn_fractf(th * pp);
  return cf{mag * __builtin_amdgcn_cosf(rev), mag * __builtin_amdgcn_sinf(rev)};
}
DEV cf csqr(cf a) { return cf{a.x * a.x - a.y * a.y, 2.f * a.x * a.y}; }
#define KG_ST(row, v) *(u32*)(bst + (row) * KG_PITCH) = pack_h((v).x, (v).y)
#define KG_STC(row, v) *(u32*)(bst + (row) * KG_PITCH) = pack_h((v).x, -(v).y)

DEV void roleB1(int bid, const unsigned short* __restrict__ lar, const unsigned short* __restrict__ aim,
                const unsigned short* __restrict__ cr, const unsigned short* __restrict__ ci,
                const unsigned short* __restrict__ logdt, const unsigned short* __restrict__ dskip, const u32* __restrict__ u32p,
                u32* __restrict__ UT, u32* __restrict__ KG, float* __restrict__ kinv, char* smem) {
  const int tid = threadIdx.x, lane = tid & 63;
  const int wv = __builtin_amdgcn_readfirstlane(tid >> 6);
  const int d0 = (bid & 7) * 128 + (bid >> 3) * 4;
  const int d = d0 + (wv >> 1);
  const int pn = d0 * 128 + tid;
  const float larv = bf_lo(lar[pn]), aimv = bf_lo(aim[pn]), crv = bf_lo(cr[pn]), civ = bf_lo(ci[pn]);
  const float ldt = bf_lo(logdt[d]);
  const float dsk = bf_lo(dskip[d]);
  const int rb = bid >> 4, cb = 2 * (bid & 7) + ((bid >> 3) & 1);
  const int cq = tid & 7, rg = (tid >> 3) & 15;
  u32x4 ur[8];
  const float dt = bf_round(expf(ldt));
  char* buf = smem + wv * KG_WAVE;
  char* bst = buf + lane * 4;
  float* kc = (float*)(buf + KG_BUF);
  float* red = (float*)(smem + 8 * KG_WAVE);
  const float LOG2E = 1.4426950408889634f, INV2PI = 0.15915494309189535f;
  const float al = (-bf_round(expf(larv)) * dt) * LOG2E;
  const float th = (aimv * dt) * INV2PI;
  cf C = cf{crv * dt, civ * dt};
  const float ssum = wave_sum(fabsf(C.x) + fabsf(C.y));
  if (lane == 0) red[wv] = ssum;
  const int ge = clampi(13 - fexp(ssum), -100, 100);
  C = cscale(C, pow2i(ge));
  const float gi = pow2i(-ge);
#define CEXP(pp) cexp_p(al, th, (float)(pp))
  asm volatile("" :: "v"(dsk));
  if (wv < 2) {
#pragma unroll
    for (int i = 0; i < 8; ++i) ur[i] = *(const u32x4*)(u32p + (size_t)(128 * rb + 8 * rg + i) * 512 + 32 * cb + 4 * cq);
  }
  const cf f1 = CEXP(8), f4_ = CEXP(32), f8 = csqr(f4_);
  {
    const cf f2_ = csqr(f1), f3 = cmul(f2_, f1), f12 = cmul(f8, f4_);
    KG_STC(0, (cf{1.f, 0.f})); KG_STC(1, f1); KG_STC(2, f2_); KG_STC(3, f3);
    KG_STC(4, f4_); KG_STC(5, cmul(f4_, f1)); KG_STC(6, cmul(f4_, f2_)); KG_STC(7, cmul(f4_, f3));
    KG_STC(8, f8); KG_STC(9, cmul(f8, f1)); KG_STC(10, cmul(f8, f2_)); KG_STC(11, cmul(f8, f3));
    KG_STC(12, f12); KG_STC(13, cmul(f12, f1)); KG_STC(14, cmul(f12, f2_)); KG_STC(15, cmul(f12, f3));
  }
  wave_sync();
  const int fr = lane & 15, fg = lane >> 4;
  const char* fptr = buf + fr * KG_PITCH + fg * 16;
  h8 F[4], G[4];
  u32 g[16];
  f4 D[3];
#define KG_RD(dst) _Pragma("unroll") for (int ks = 0; ks < 4; ++ks) dst[ks] = *(const h8*)(fptr + ks * 64);
#define KG_PUT(n) wave_sync(); _Pragma("unroll") for (int rr = 0; rr < (n); ++rr) *(u32*)(bst + rr * KG_PITCH) = g[rr]; wave_sync();
#define KG_MM(b) D[b] = Z4; _Pragma("unroll") for (int ks = 0; ks < 4; ++ks) D[b] = mfma_h(G[ks], F[ks], D[b]);
#define PH(v) pack_h((v).x, (v).y)
  KG_RD(F)
  const cf e1 = CEXP(1), e2 = csqr(e1), e4 = csqr(e2);
  const cf e3 = cmul(e1, e2), e5 = cmul(e4, e1), e6 = cmul(e4, e2), e7 = cmul(e4, e3);
  const cf h1 = csqr(f8), h2_ = CEXP(256), h4 = csqr(h2_);
  const cf h3 = cmul(h1, h2_);
  {
    const cf H1 = cmul(C, h1);
    g[0] = PH(C); g[1] = PH(cmul(C, e1)); g[2] = PH(cmul(C, e2)); g[3] = PH(cmul(C, e3));
    g[4] = PH(cmul(C, e4)); g[5] = PH(cmul(C, e5)); g[6] = PH(cmul(C, e6)); g[7] = PH(cmul(C, e7));
    g[8] = PH(H1); g[9] = PH(cmul(H1, e1)); g[10] = PH(cmul(H1, e2)); g[11] = PH(cmul(H1, e3));
    g[12] = PH(cmul(H1, e4)); g[13] = PH(cmul(H1, e5)); g[14] = PH(cmul(H1, e6)); g[15] = PH(cmul(H1, e7));
  }
  KG_PUT(16)
  KG_RD(G)
  const cf H4 = cmul(C, h4);
  {
    const cf H2 = cmul(C, h2_), H3 = cmul(C, h3);
    g[0] = PH(H2); g[1] = PH(cmul(H2, e2)); g[2] = PH(cmul(H2, e4)); g[3] = PH(cmul(H2, e6));
    g[4] = PH(H3); g[5] = PH(cmul(H3, e2)); g[6] = PH(cmul(H3, e4)); g[7] = PH(cmul(H3, e6));
    const cf H5 = cmul(H4, h1), H6 = cmul(H4, h2_), H7 = cmul(H4, h3);
    g[8] = PH(H4); g[9] = PH(cmul(H4, e4)); g[10] = PH(H5); g[11] = PH(cmul(H5, e4));
    g[12] = PH(H6); g[13] = PH(cmul(H6, e4)); g[14] = PH(H7); g[15] = PH(cmul(H7, e4));
  }
  KG_MM(0)
  KG_PUT(16)
  KG_RD(G)
  {
    const cf H8 = cmul(H4, h4), H12 = cmul(H8, h4), H16 = cmul(H12, h4);
    g[0] = PH(H8); g[1] = PH(cmul(H8, h1)); g[2] = PH(cmul(H8, h2_)); g[3] = PH(cmul(H8, h3));
    g[4] = PH(H12); g[5] = PH(cmul(H12, h1)); g[6] = PH(cmul(H12, h2_)); g[7] = PH(cmul(H12, h3));
    g[8] = PH(H16);
  }
  KG_MM(1)
  KG_PUT(9)
  KG_RD(G)
  KG_MM(2)
  {
    const int col = fr, q = fg;
    D[0] *= gi; D[1] *= gi; D[2] *= gi;
    *(f4*)(kc + 128 * (q >> 1) + 4 * (q & 1) + 8 * col) = D[0];
    if (q < 2) {
      *(f4*)(kc + 256 + 64 * q + 4 * col) = D[1];
#pragma unroll
      for (int rg2 = 0; rg2 < 4; ++rg2) kc[512 + 16 * (4 * q + rg2) + col] = D[2][rg2];
    } else {
      float* b2 = kc + 384 + 64 * (q - 2) + 2 * col;
      b2[0] = D[1][0]; b2[1] = D[1][1]; b2[32] = D[1][2]; b2[33] = D[1][3];
      if (q == 2 && col < 8) kc[640 + col] = D[2][0];
    }
  }
  char* tt = smem + 8 * KG_WAVE + 64;
  if (wv < 2) {
#pragma unroll
    for (int q = 0; q < 4; ++q) {
      u32x4 lo, hi;
#pragma unroll
      for (int i = 0; i < 4; ++i) {
        lo[i] = __builtin_amdgcn_perm(ur[2 * i + 1][q], ur[2 * i][q], 0x05040100);
        hi[i] = __builtin_amdgcn_perm(ur[2 * i + 1][q], ur[2 * i][q], 0x07060302);
      }
      char* dp = tt + (8 * cq + 2 * q) * 256 + 16 * (rg ^ cq);
      *(u32x4*)dp = lo;
      *(u32x4*)(dp + 256) = hi;
    }
  }
  __syncthreads();
  {
    const int c = tid >> 7, X = tid & 127;
    const int fe = clampi(13 - fexp(red[2 * c] + red[2 * c + 1]), -100, 100);
    const float fs = pow2i(fe);
    if (X < 81) {
      const float* ka = (const float*)(smem + (2 * c) * KG_WAVE + KG_BUF) + 8 * X;
      const float* kb2 = (const float*)(smem + (2 * c + 1) * KG_WAVE + KG_BUF) + 8 * X;
      const f4 s0 = (*(const f4*)ka + *(const f4*)kb2) * fs, s1 = (*(const f4*)(ka + 4) + *(const f4*)(kb2 + 4)) * fs;
      st128<ST_AUX_B>(mkrsrc(KG), (d0 + c) * KC_DW + 4 * X,
            u32x4{pack_h(s0.x, s0.y), pack_h(s0.z, s0.w), pack_h(s1.x, s1.y), pack_h(s1.z, s1.w)});
    }
    if (X == 127) { kinv[2 * (d0 + c)] = 2.f * pow2i(-fe); kinv[2 * (d0 + c) + 1] = dsk; }
  }
#pragma unroll
  for (int it = 0; it < 2; ++it) {
    const int t2 = tid + 512 * it, ch = t2 >> 4, ck = t2 & 15;
    st128<ST_AUX_B>(mkrsrc(UT), (64 * cb + ch) * 1024 + 64 * rb + 4 * ck, *(const u32x4*)(tt + ch * 256 + 16 * (ck ^ (ch >> 3))));
  }
}

static constexpr int CV_KC = 4224;
static constexpr int CV_UP = 128;
static constexpr int CV_CH = 2 * CV_KC + 71 * CV_UP + 1408;
static constexpr int B2_LDS = 4 * CV_CH + 64;

DEV void roleB2(int bid, const u32* UT, const u32* __restrict__ KG, const float* __restrict__ kinv,
                const unsigned short* __restrict__ dskip, u32* YT, char* smem) {
  const int tid = threadIdx.x, lane = tid & 63;
  const int wv = __builtin_amdgcn_readfirstlane(tid >> 6);
  __builtin_amdgcn_s_setprio(PRIO_B0);
  const int ch = wv >> 1, hw = wv & 1;
  const int d = (bid & 7) * 128 + (bid >> 3) * 4 + ch;
  const u32* ut = UT + (size_t)d * 1024;
  char* kb = smem + ch * CV_CH;
  char* ub = kb + 2 * CV_KC;
  int* uex = (int*)(smem + 4 * CV_CH);
#define PK2(lo, hi) ((u32)(lo) | ((u32)(hi) << 16))
  const int fr = lane & 15, fg = lane >> 4;
  const int nu = fr & 1;
  const int lw = 16 * (fr >> 1) + 4 * fg + 2 * nu;
  u32x2 uu[4];
  if (hw == 0) {
    const u32* kg = KG + (size_t)d * KC_DW;
    const u32x4 kc0 = *(const u32x4*)(kg + 4 * lane);
    const u32x4 kc1 = *(const u32x4*)(kg + 256 + 4 * (lane < 17 ? lane : 0));
#pragma unroll
    for (int t = 0; t < 4; ++t) uu[t] = *(const u32x2*)(ut + 128 * t + lw);
    unsigned short* kc = (unsigned short*)(ub + 71 * CV_UP + 16);
    *(u32x4*)((char*)kc + 16 * lane) = kc0;
    if (lane < 17) *(u32x4*)((char*)kc + 1024 + 16 * lane) = kc1;
    if (lane >= 32 && lane < 40) *(u32x4*)(kb + ((lane >> 2) & 1) * CV_KC + 4096 + (lane & 3) * 16) = u32x4{0u, 0u, 0u, 0u};
    wave_sync();
#pragma unroll
    for (int tt = 0; tt < 2; ++tt) {
      const int X = lane + 64 * tt;
      const int mp = 255 - X;
      const u32 a = lds_rd16(kc + 384 + mp), b = lds_rd16(kc + 385 + mp);
      const u32 x = (mp & 1) ? b : a;
      const u32 aa = PK2(a, a), bb = PK2(b, b);
      *(u32x4*)(kb + CV_KC + 16 * X) = u32x4{bb, PK2(b, x), aa, aa};
      *(u32x4*)(kb + 16 * X) = u32x4{bb, PK2(x, a), aa, aa};
    }
    {
      const int X = lane + 128;
      const int m4 = 2 * (255 - X);
      const u32 ab = *(const u32*)(kc + 256 + m4), c = kc[258 + m4];
      const u32 a = ab & 0xffffu, b = ab >> 16;
      const u32 aa = PK2(a, a), bb = PK2(b, b);
      *(u32x4*)(kb + CV_KC + 16 * X) = u32x4{PK2(c, c), bb, PK2(b, a), aa};
      *(u32x4*)(kb + 16 * X) = u32x4{PK2(c, b), bb, aa, aa};
    }
    {
      const int X = lane + 192;
      const int l0 = 2040 - 8 * X;
      u32x4 c1, c0;
      if (lane < 32) {
        const unsigned short* sp = kc + 128 + (l0 >> 1);
        const u32x2 w = *(const u32x2*)sp;
        const u32 a = w.x & 0xffffu, b = w.x >> 16, c = w.y & 0xffffu, dd = w.y >> 16, e = sp[4];
        const u32 nx = lane == 31 ? (u32)sp[-1] : a;
        c1 = u32x4{PK2(e, dd), PK2(c, c), PK2(c, b), PK2(a, a)};
        c0 = u32x4{PK2(dd, c), PK2(c, c), PK2(b, a), PK2(a, nx)};
      } else {
        const u32x4 w = *(const u32x4*)(kc + l0);
        const u32 pv = lane == 63 ? 0u : ((u32)kc[l0 - 1] << 16);
        c1 = u32x4{__builtin_amdgcn_alignbit(w.w, w.w, 16), __builtin_amdgcn_alignbit(w.z, w.z, 16),
                   __builtin_amdgcn_alignbit(w.y, w.y, 16), __builtin_amdgcn_alignbit(w.x, w.x, 16)};
        c0 = u32x4{__builtin_amdgcn_perm(w.z, w.w, 0x07060100), __builtin_amdgcn_perm(w.y, w.z, 0x07060100),
                   __builtin_amdgcn_perm(w.x, w.y, 0x07060100), __builtin_amdgcn_perm(pv, w.x, 0x07060100)};
      }
      *(u32x4*)(kb + CV_KC + 16 * X) = c1;
      *(u32x4*)(kb + 16 * X) = c0;
    }
  } else {
    u32x4 uw[4];
    u32 um[4];
#pragma unroll
    for (int tt = 0; tt < 4; ++tt) {
      const int X = lane + 64 * tt;
      uw[tt] = *(const u32x4*)(ut + 4 * X);
      um[tt] = ut[X ? 4 * X - 1 : 0];
    }
#pragma unroll
    for (int t = 0; t < 4; ++t) uu[t] = *(const u32x2*)(ut + 128 * (4 + t) + lw);
    if (lane == 0) um[0] = 0;
    const u32x4 z = {0u, 0u, 0u, 0u};
    if (lane < 56) *(u32x4*)(ub + lane * 16) = z;
    float fm = 0.f;
#pragma unroll
    for (int tt = 0; tt < 4; ++tt)
#pragma unroll
      for (int q = 0; q < 4; ++q) fm = fmaxf(fm, fmaxf(fabsf(bf_lo(uw[tt][q])), fabsf(bf_hi(uw[tt][q]))));
    fm = wave_max(fm);
    const int ue = clampi(14 - fexp(fm), -100, 100);
    const float us = pow2i(ue);
    if (lane == 0) uex[ch] = ue;
#pragma unroll
    for (int tt = 0; tt < 4; ++tt) {
      const int X = lane + 64 * tt;
      const u32 hm = pack_h(bf_lo(um[tt]) * us, bf_hi(um[tt]) * us);
      u32 h[4];
#pragma unroll
      for (int q = 0; q < 4; ++q) h[q] = pack_h(bf_lo(uw[tt][q]) * us, bf_hi(uw[tt][q]) * us);
      const int Rp = (X >> 2) + 7, gg = X & 3;
      const int off = Rp * CV_UP + 32 * ((2 * (gg >> 1)) ^ ((Rp >> 1) & 3)) + 16 * (gg & 1);
      *(u32x4*)(ub + off) = u32x4{hm, h[0], h[0], h[1]};
      *(u32x4*)(ub + (off ^ 32)) = u32x4{h[1], h[2], h[2], h[3]};
    }
  }
  __builtin_amdgcn_s_setprio(PRIO_B1);
  __syncthreads();
  const float kiv = kinv[2 * d];
  const float dsk = kinv[2 * d + 1];
  const char* aB = kb + (fr & 1) * CV_KC + 2 * (2044 - 4 * (fr >> 1) + 8 * fg) - 64 * 63 + 64 * (1 - hw);
  f4 acc[8];
#pragma unroll
  for (int T = 0; T < 8; ++T) acc[T] = Z4;
  {
    const char* bB0[4];
    const char* bB1[4];
#pragma unroll
    for (int i2 = 0; i2 < 4; ++i2) {
      const int Rl = (fr >> 1) + 7 - hw - 2 * i2;
      const int bo = Rl * CV_UP + 32 * ((2 * (fg >> 1)) ^ ((Rl >> 1) & 3)) + 8 * (2 * (fg & 1) + (fr & 1));
      bB0[i2] = ub + bo; bB1[i2] = ub + (bo ^ 32);
    }
#define LDA(i2, e) ldfrag2(aB + 64 * (62 - (2 * (i2) + 8 * (e))), aB + 64 * (62 - (2 * (i2) + 8 * (e))) + 8)
#define LDB(i2, q) ldfrag2(bB0[i2] + 8 * CV_UP * (q), bB1[i2] + 8 * CV_UP * (q))
#define CV_PD 4
    h8 B[8];
    h8 AR[CV_PD + 1];
    AR[0] = LDA(0, 0);
#pragma unroll
    for (int q = 7; q >= 0; --q) B[q] = LDB(0, q);
#pragma unroll
    for (int k = 1; k < CV_PD; ++k) AR[k] = LDA(0, k);
#pragma unroll
    for (int st = 0; st < 32; ++st) {
      const int i2 = st >> 3, e = st & 7;
      if (st + CV_PD < 32) AR[(st + CV_PD) % (CV_PD + 1)] = LDA((st + CV_PD) >> 3, (st + CV_PD) & 7);
#pragma unroll
      for (int T = 7; T >= e; --T) acc[T] = mfma_h(AR[st % (CV_PD + 1)], B[T - e], acc[T]);
      if (i2 < 3) B[7 - e] = LDB(i2 + 1, 7 - e);
    }
  }
  const float osc = kiv * pow2i(-uex[ch]);
  __syncthreads();
#pragma unroll
  for (int t = 0; t < 4; ++t) {
    *(f4*)(kb + (hw * 4 + t) * 1024 + lane * 16) = fsel(hw, acc[t], acc[4 + t]);
    acc[t] = fsel(hw, acc[4 + t], acc[t]);
  }
  __syncthreads();
#pragma unroll
  for (int t = 0; t < 4; ++t) {
    const f4 a = acc[t] + *(const f4*)(kb + ((1 - hw) * 4 + t) * 1024 + lane * 16);
    const u32 p01 = pack_bf(a[0] * osc, a[1] * osc), p23 = pack_bf(a[2] * osc, a[3] * osc);
    const u32 rcv = dpp_xor1(nu ? p01 : p23);
    const u32 lo = nu ? rcv : p01, hi = nu ? p23 : rcv;
    const u32 s0 = pack_bf(bf_lo(uu[t].x) * dsk, bf_hi(uu[t].x) * dsk), s1 = pack_bf(bf_lo(uu[t].y) * dsk, bf_hi(uu[t].y) * dsk);
    u32x2 o;
    o.x = pack_bf(bf_lo(lo) + bf_lo(s0), bf_hi(lo) + bf_hi(s0));
    o.y = pack_bf(bf_lo(hi) + bf_lo(s1), bf_hi(hi) + bf_hi(s1));
    st64<ST_AUX_B>(mkrsrc(YT), d * 1024 + 128 * (4 * hw + t) + lw, o);
  }
}

DEV void roleB3(int bid, const u32* __restrict__ YT, u32* __restrict__ out32, char* smem) {
  const int tid = threadIdx.x;
  const int rb = bid >> 3, cb = 2 * (bid & 7) + (tid >> 8);
  const int cq = tid & 7, rg = (tid >> 3) & 31;
  u32x4 ld[4];
#pragma unroll
  for (int it = 0; it < 4; ++it) {
    const int t2 = tid + 512 * it;
    ld[it] = *(const u32x4*)(YT + (size_t)(128 * (bid & 7) + (t2 >> 4)) * 1024 + 64 * rb + 4 * (t2 & 15));
  }
#pragma unroll
  for (int it = 0; it < 4; ++it) {
    const int t2 = tid + 512 * it, ch = t2 >> 4;
    *(u32x4*)(smem + ch * 256 + 16 * ((t2 & 15) ^ ((ch >> 3) & 7))) = ld[it];
  }
  __syncthreads();
  u32x2 y[8];
#pragma unroll
  for (int q = 0; q < 8; ++q) y[q] = *(const u32x2*)(smem + (64 * (tid >> 8) + 8 * cq + q) * 256 + 16 * ((rg >> 1) ^ cq) + 8 * (rg & 1));
#pragma unroll
  for (int i = 0; i < 4; ++i) {
    u32x4 o;
#pragma unroll
    for (int q = 0; q < 4; ++q) {
      const u32 a = (i & 2) ? y[2 * q].y : y[2 * q].x, b = (i & 2) ? y[2 * q + 1].y : y[2 * q + 1].x;
      o[q] = __builtin_amdgcn_perm(b, a, (i & 1) ? 0x07060302 : 0x05040100);
    }
    st128<ST_AUX_OUTB>(mkrsrc(out32), (128 * rb + 4 * rg + i) * 1024 + 512 + 32 * cb + 4 * cq, o);
  }
}

static constexpr int K1_LDS = B1_LDS > AC_LDS ? B1_LDS : AC_LDS;
static constexpr int K2_LDS = B2_LDS > A2_LDS ? B2_LDS : A2_LDS;
static constexpr int K3_LDS = AC_LDS;
typedef unsigned short us16;
#define BOFF 0

#define SYM_ADDR(var, sym)                                                                       \
  unsigned long long var;                                                                        \
  {                                                                                              \
    u32 lo_, hi_;                                                                                \
    asm volatile("s_getpc_b64 s[30:31]\n s_add_u32 %0, s30, " #sym "@rel32@lo+4\n s_addc_u32 %1, s31, " #sym "@rel32@hi+12" \
                 : "=s"(lo_), "=s"(hi_) : : "s30", "s31", "scc");                                 \
    var = ((unsigned long long)hi_ << 32) | lo_;                                                 \
  }
extern "C" __global__ NOPK __launch_bounds__(512, 4) void gfs4d_k1(const u32* __restrict__ x, const u32* __restrict__ u, const us16* __restrict__ lar,
                                             const us16* __restrict__ aim, const us16* __restrict__ cr, const us16* __restrict__ ci,
                                             const us16* __restrict__ logdt, const us16* __restrict__ dskip, const u32* __restrict__ wr, const u32* __restrict__ wi,
                                             char* __restrict__ ws, u32* __restrict__ out, int boff) {
  __shared__ __attribute__((aligned(16))) char smem[K1_LDS];
  const int b = blockIdx.x + BOFF;
  u32x4 cpf = {0u, 0u, 0u, 0u};
  {
    SYM_ADDR(c0, gfs4d_k1)
    SYM_ADDR(c1, gfs4d_end)
    const unsigned long long len = c1 - c0, off = (unsigned long long)(((b & 255) >> 3) * 512 + threadIdx.x) * 16;
    if (b >= 256 && len < (1u << 17) && off + 16 <= len) cpf = *(const u32x4*)(c0 + off);
  }
  if (b >= 256) roleAC<true>(b - 256, x, nullptr, (u32*)(ws + WS_V1), (int*)(ws + WS_S1), smem, wr, wi);
  else roleB1(b, lar, aim, cr, ci, logdt, dskip, u, (u32*)(ws + WS_UT), (u32*)(ws + WS_KG), (float*)(ws + WS_KI), smem);
  asm volatile("" :: "v"(cpf));
}
extern "C" __global__ NOPK __launch_bounds__(512, 4) void gfs4d_k2(const us16* __restrict__ wr, const us16* __restrict__ wi,
                                             const us16* __restrict__ dskip, char* __restrict__ ws, int boff) {
  __shared__ __attribute__((aligned(128))) char smem[K2_LDS];
  const int b = blockIdx.x + BOFF;
  if (b >= 256)
    roleA2(b - 256, (const u32*)(ws + WS_V1), (const int*)(ws + WS_S1), wr, wi, (u32*)(ws + WS_V1), (int*)(ws + WS_S2), smem);
  else
    roleB2(b, (const u32*)(ws + WS_UT), (const u32*)(ws + WS_KG), (const float*)(ws + WS_KI), dskip, (u32*)(ws + WS_UT), smem);
}
extern "C" __global__ NOPK __launch_bounds__(512, 4) void gfs4d_k3(char* __restrict__ ws, u32* __restrict__ out, int boff) {
  __shared__ __attribute__((aligned(16))) char smem[K3_LDS];
  const int b = blockIdx.x + BOFF;
  if (b < 256) roleAC<false>(b, (const u32*)(ws + WS_V1), (const int*)(ws + WS_S2), out, nullptr, smem);
  else roleB3(b - 256, (const u32*)(ws + WS_UT), out, smem);
}

extern "C" __global__ void gfs4d_end() {}

static void launch_part(int which, int roles, void* const* d_in, void* d_out, void* d_ws, hipStream_t stream) {
  char* ws = (char*)d_ws;
  if (which == 1) {
    const int grid = roles == 3 ? 512 : 256, boff = roles == 1 ? 256 : 0;
    gfs4d_k1<<<grid, 512, 0, stream>>>((const u32*)d_in[0], (const u32*)d_in[3], (const us16*)d_in[4], (const us16*)d_in[5],
                                 (const us16*)d_in[6], (const us16*)d_in[7], (const us16*)d_in[8], (const us16*)d_in[9], (const u32*)d_in[1], (const u32*)d_in[2], ws, (u32*)d_out, boff);
  } else if (which == 2) {
    const int grid = roles == 3 ? 512 : 256, boff = roles == 1 ? 256 : 0;
    gfs4d_k2<<<grid, 512, 0, stream>>>((const us16*)d_in[1], (const us16*)d_in[2], (const us16*)d_in[9], ws, boff);
  } else {
    const int grid = roles == 3 ? 384 : (roles == 1 ? 256 : 128), boff = roles == 2 ? 256 : 0;
    gfs4d_k3<<<grid, 512, 0, stream>>>(ws, (u32*)d_out, boff);
  }
}

extern "C" void kernel_launch(void* const* d_in, const int* in_sizes, int n_in, void* d_out, int out_size, void* d_ws,
                              size_t ws_size, hipStream_t stream) {
  launch_part(1, 3, d_in, d_out, d_ws, stream);
  launch_part(2, 3, d_in, d_out, d_ws, stream);
  launch_part(3, 3, d_in, d_out, d_ws, stream);
}
```
